```python
import torch

def _softmax(x, axis=-1):
    return torch.softmax(x, dim=axis)
def _gelu(x):
    return torch.nn.functional.gelu(x, approximate="tanh")
def _silu(x):
    return torch.nn.functional.silu(x)
def _sigmoid(x):
    return torch.sigmoid(x)
def _layernorm(x, axis=-1, eps=1e-5):
    m = x.mean(dim=axis, keepdim=True)
    v = x.var(dim=axis, keepdim=True, unbiased=False)
    return (x - m) / torch.sqrt(v + eps)
def _scatter_add(tgt, idx, src, axis=0):
    return tgt.clone().index_add_(axis, idx, src)
def _segment_sum(src, seg, n):
    out = torch.zeros((n,) + src.shape[1:], dtype=src.dtype)
    return out.index_add_(0, seg, src)
def _pad(x, pw):
    flat = tuple(v for pair in reversed(pw) for v in pair)
    return torch.nn.functional.pad(x, flat)

kernel_name = 'gated_mix__gqa_block_cached__dense_activation_silu__decode'

def setup_inputs():

    _w = (lambda a: (lambda t: t.to(torch.bfloat16) if t.is_floating_point() else t)(torch.as_tensor(a)))
    def _A_setup():
        B, Sq, D = 4, 1, 640
        Hq, Hkv, Sk, Dh = 32, 32, 4096, 192
        assert Hq % Hkv == 0, (Hq, Hkv)
        return {
            "x": _w((lambda *s: torch.randn(*s, dtype=torch.bfloat16) * 0.1)(B, Sq, D)),
            "k_cache": _w((lambda *s: torch.randn(*s, dtype=torch.bfloat16) * 3.2)(B, Hkv, Sk, Dh)),
            "v_cache": _w((lambda *s: torch.randn(*s, dtype=torch.bfloat16) * 3.2)(B, Hkv, Sk, Dh)),
            "wq": _w((lambda *s: torch.randn(*s, dtype=torch.bfloat16) * 3.2)(D, Hq * Dh) * (D ** -0.5)),
            "wo": _w((lambda *s: torch.randn(*s, dtype=torch.bfloat16) * 3.2)(Hq * Dh, D) * ((Hq * Dh) ** -0.5)),
        }
    _A_in = _A_setup()

    def _B_setup():
        B, S, D, F = 4, 1, 640, 1536
        return {
            "x": _w((lambda *s: torch.randn(*s, dtype=torch.bfloat16) * 0.1)(B, S, D)),
            "w1": _w((lambda *s: torch.randn(*s, dtype=torch.bfloat16) * 3.2)(D, F) * (D ** -0.5)),
            "w2": _w((lambda *s: torch.randn(*s, dtype=torch.bfloat16) * 3.2)(F, D) * (F ** -0.5)),
        }
    _B_in = _B_setup()

    return {
        "A_x": _A_in["x"],
        "A_k_cache": _A_in["k_cache"],
        "A_v_cache": _A_in["v_cache"],
        "A_wq": _A_in["wq"],
        "A_wo": _A_in["wo"],
        "B_x": _B_in["x"],
        "B_w1": _B_in["w1"],
        "B_w2": _B_in["w2"],
        "gate_w": _w((lambda *s: torch.randn(*s, dtype=torch.bfloat16) * 0.1)(640) * 16.0),
    }

def reference(A_x, A_k_cache, A_v_cache, A_wq, A_wo, B_x, B_w1, B_w2, gate_w):
    def _A_ref(x, k_cache, v_cache, wq, wo):
        B, Sq, D = x.shape
        Hq, Dh = 32, 192
        Hkv = k_cache.shape[1]
        rep = Hq // Hkv
        q = (lambda x, *d: x.permute(*d))((x @ wq).reshape(B, Sq, Hq, Dh), 0, 2, 1, 3)
        k = (lambda x, n, axis: torch.repeat_interleave(x, n, dim=axis))(k_cache, rep, axis=1)
        v = (lambda x, n, axis: torch.repeat_interleave(x, n, dim=axis))(v_cache, rep, axis=1)
        scores = torch.einsum("bhqd,bhkd->bhqk", q, k) * (Dh ** -0.5)
        p = _softmax(scores, axis=-1)
        o = torch.einsum("bhqk,bhkd->bhqd", p, v)
        return (lambda x, *d: x.permute(*d))(o, 0, 2, 1, 3).reshape(B, Sq, Hq * Dh) @ wo

    def _B_ref(x, w1, w2):
        h = torch.einsum("bsd,df->bsf", x, w1)
        h = _silu(h)
        out = torch.einsum("bsf,fd->bsd", h, w2)
        return out

    x = A_x
    g = _sigmoid((x * gate_w).sum(-1, keepdims=True))
    a = _A_ref(x, A_k_cache, A_v_cache, A_wq, A_wo)
    b = _B_ref(x, B_w1, B_w2)
    return g * a + (1 - g) * b
```

```cpp
#include <hip/hip_runtime.h>
#include <hip/hip_bf16.h>
#include <stdint.h>

typedef unsigned short u16;
typedef unsigned int u32;
typedef unsigned long long u64;
using u32x4 = __attribute__((ext_vector_type(4))) unsigned;
using u32x2 = __attribute__((ext_vector_type(2))) unsigned;
using f32x4 = __attribute__((ext_vector_type(4))) float;
using bf16x2 = __attribute__((ext_vector_type(2))) __bf16;
using bf16x8 = __attribute__((ext_vector_type(8))) __bf16;
typedef u32x4 u32x4_a4 __attribute__((aligned(4)));
typedef __attribute__((address_space(1))) u64 gu64;

#define KV_NT 1

constexpr int DM = 640, NH = 32, SK = 4096, DH = 192, FF = 1536, QD = NH * DH;
constexpr int NTHR = 512;
constexpr int NT = 8;
constexpr int WTS = 264;
constexpr int WS_EPOCH = 0;
constexpr int WS_F1 = 4096;
constexpr int WS_F2 = WS_F1 + 256 * 128;
constexpr int WS_P1 = WS_F2 + 256 * 128;
constexpr int WS_P2 = WS_P1 + 256 * 1024;
constexpr int WS_END = WS_P2 + 256 * 1280;
constexpr unsigned SPIN_CAP = 1u << 20;

#define RLX_AGENT __ATOMIC_RELAXED, __HIP_MEMORY_SCOPE_AGENT
#define WG_BARRIER() asm volatile("s_waitcnt lgkmcnt(0)\n\ts_barrier" ::: "memory")
#define PIN() do { asm volatile("" ::: "memory"); __builtin_amdgcn_sched_barrier(0); } while (0)

#define PIN_ACC(a) asm volatile("" : "+v"(a[0]), "+v"(a[1]), "+v"(a[2]), "+v"(a[3]), "+v"(a[4]), "+v"(a[5]), "+v"(a[6]), "+v"(a[7]), \
    "+v"(a[8]), "+v"(a[9]), "+v"(a[10]), "+v"(a[11]), "+v"(a[12]), "+v"(a[13]), "+v"(a[14]), "+v"(a[15]), \
    "+v"(a[16]), "+v"(a[17]), "+v"(a[18]), "+v"(a[19]), "+v"(a[20]), "+v"(a[21]), "+v"(a[22]), "+v"(a[23]) :: "memory")

__device__ __forceinline__ float dot2(unsigned k, unsigned q, float acc) {
  return __builtin_amdgcn_fdot2_f32_bf16(__builtin_bit_cast(bf16x2, k), __builtin_bit_cast(bf16x2, q), acc, false);
}
__device__ __forceinline__ unsigned cvtpk(float lo, float hi) {
  unsigned r; asm("v_cvt_pk_bf16_f32 %0, %1, %2" : "=v"(r) : "v"(lo), "v"(hi)); return r;
}
__device__ __forceinline__ float bf_lo(unsigned w) { return __builtin_bit_cast(float, w << 16); }
__device__ __forceinline__ float bf_hi(unsigned w) { return __builtin_bit_cast(float, w & 0xffff0000u); }
__device__ __forceinline__ float rbf(float x) { return bf_lo(cvtpk(x, x)); }
template <int CTRL> __device__ __forceinline__ float dpp(float x) {
  return __builtin_bit_cast(float, __builtin_amdgcn_mov_dpp(__builtin_bit_cast(int, x), CTRL, 0xf, 0xf, true));
}
constexpr int XOR1 = 0xB1, XOR2 = 0x4E, XOR7 = 0x141, ROR8 = 0x128;
__device__ __forceinline__ float xrow16_max(float x) {
  auto s = __builtin_amdgcn_permlane16_swap(__float_as_uint(x), __float_as_uint(x), false, false);
  x = fmaxf(__uint_as_float(s[0]), __uint_as_float(s[1]));
  auto t = __builtin_amdgcn_permlane32_swap(__float_as_uint(x), __float_as_uint(x), false, false);
  return fmaxf(__uint_as_float(t[0]), __uint_as_float(t[1]));
}
__device__ __forceinline__ float xrow16_sum(float x) {
  auto s = __builtin_amdgcn_permlane16_swap(__float_as_uint(x), __float_as_uint(x), false, false);
  x = __uint_as_float(s[0]) + __uint_as_float(s[1]);
  auto t = __builtin_amdgcn_permlane32_swap(__float_as_uint(x), __float_as_uint(x), false, false);
  return __uint_as_float(t[0]) + __uint_as_float(t[1]);
}
__device__ __forceinline__ float rsum8(float x) { x += dpp<ROR8>(x); return xrow16_sum(x); }
__device__ __forceinline__ float rmax8(float x) { x = fmaxf(x, dpp<ROR8>(x)); return xrow16_max(x); }
__device__ __forceinline__ float csum8(float x) { x += dpp<XOR1>(x); x += dpp<XOR2>(x); x += dpp<XOR7>(x); return x; }
typedef __amdgpu_buffer_rsrc_t rsrc_t;
__device__ __forceinline__ rsrc_t mk_rsrc(const void* p, unsigned bytes) { return __builtin_amdgcn_make_buffer_rsrc((void*)p, 0, bytes, 0x00020000); }
__device__ __forceinline__ u32x4 ld_kv(rsrc_t rs, int voff, int soff) { return __builtin_amdgcn_raw_buffer_load_b128(rs, voff, soff, KV_NT ? 2 : 0); }
__device__ __forceinline__ u32x4 ld_w(rsrc_t rs, int voff, int soff) { return __builtin_amdgcn_raw_buffer_load_b128(rs, voff, soff, 0); }
__device__ __forceinline__ u32x4 ld16(const u16* p) { return *reinterpret_cast<const u32x4*>(p); }
__device__ __forceinline__ void wait_flags(const unsigned char* base, int n, int lane, u64 tag) {
  gu64* f = (gu64*)(base + (size_t)(lane < n ? lane : 0) * 128);
  for (unsigned spins = 0; spins < SPIN_CAP; ++spins) {
    const u64 v = __hip_atomic_load(f, RLX_AGENT);
    if (__all(v == tag)) break;
    __builtin_amdgcn_s_sleep(1);
  }
  asm volatile("" ::: "memory");
}

__global__ void __launch_bounds__(NTHR, 2)
fused_decode(const u16* __restrict__ x, const u16* __restrict__ kc, const u16* __restrict__ vc, const u16* __restrict__ wq,
             const u16* __restrict__ wo, const u16* __restrict__ w1, const u16* __restrict__ w2, const u16* __restrict__ gw,
             u16* __restrict__ out, unsigned char* __restrict__ ws) {
  __shared__ __attribute__((aligned(16))) float xs[DM * 4];
  __shared__ __attribute__((aligned(16))) u16 wt[80 * WTS];
  __shared__ __attribute__((aligned(16))) float red[8 * 320];
  __shared__ __attribute__((aligned(16))) float wp[8 * 196];
  __shared__ __attribute__((aligned(16))) u16 qs[DH];
  __shared__ __attribute__((aligned(16))) u16 hs[4 * 64];
  __shared__ __attribute__((aligned(16))) u16 us[4 * DH];
  __shared__ __attribute__((aligned(16))) float fb[320];
  __shared__ __attribute__((aligned(16))) float gs[8];

  const int bid = blockIdx.x, tid = threadIdx.x, lane = tid & 63;
  const int wave = __builtin_amdgcn_readfirstlane(tid >> 6);
  const int r = lane >> 3, c = lane & 7;
  const int h = (bid & 7) * 4 + (bid >> 6), mem = (bid >> 3) & 7, b = mem >> 1, half = mem & 1, g = mem;

  const u16* xp = x + b * DM + 80 * wave + 10 * r;
  const u32x4 xa = *reinterpret_cast<const u32x4_a4*>(xp);
  const u32 xb = *reinterpret_cast<const u32*>(xp + 8);
  const rsrc_t rwq = mk_rsrc(wq + h * DH, (DM * QD - h * DH) * 2);
  u32x4 W[10][3];
  {
    const int vo = (10 * r * QD + c * 8) * 2, so = 80 * wave * QD * 2;
#pragma unroll
    for (int i = 0; i < 10; ++i)
#pragma unroll
      for (int j = 0; j < 3; ++j) W[i][j] = ld_w(rwq, vo + j * 128, so + i * QD * 2);
  }
  PIN();
  const size_t kvoff = ((size_t)(b * NH + h) * SK + half * 2048) * DH;
  const rsrc_t rk = mk_rsrc(kc + kvoff, 2048 * DH * 2), rv = mk_rsrc(vc + kvoff, 2048 * DH * 2);
  const int kvo = r * (DH * 2) + c * 16, kso = wave * (32 * DH * 2);
  u32x4 K[4][3], V[4][3];
#define LD_TILE(T, RS, TI) _Pragma("unroll") for (int i = 0; i < 4; ++i) _Pragma("unroll") for (int j = 0; j < 3; ++j) \
    T[i][j] = ld_kv(RS, kvo + (i & 1) * 3072 + j * 128, kso + (TI) * (256 * DH * 2) + (i >> 1) * 6144)
  LD_TILE(K, rk, 0);
  PIN();
  float acc[24];
#pragma unroll
  for (int k = 0; k < 24; ++k) acc[k] = 0.f;
  const u32 xw[5] = {xa[0], xa[1], xa[2], xa[3], xb};
#define Q_ROWS(I0, I1) _Pragma("unroll") for (int i = (I0); i < (I1); ++i) { const float xv = (i & 1) ? bf_hi(xw[i >> 1]) : bf_lo(xw[i >> 1]); \
    _Pragma("unroll") for (int j = 0; j < 3; ++j) _Pragma("unroll") for (int e = 0; e < 4; ++e) { \
      acc[j * 8 + 2 * e] = fmaf(xv, bf_lo(W[i][j][e]), acc[j * 8 + 2 * e]); acc[j * 8 + 2 * e + 1] = fmaf(xv, bf_hi(W[i][j][e]), acc[j * 8 + 2 * e + 1]); } }
  Q_ROWS(0, 5)
  PIN_ACC(acc);
  PIN();
  LD_TILE(V, rv, 0);
  PIN();
  Q_ROWS(5, 10)
#undef Q_ROWS
  PIN_ACC(acc);
  PIN();
  u32x4 WS[5], XS;
  {
#pragma unroll
    for (int i = 0; i < 5; ++i) {
      int ch = tid + i * NTHR; ch = ch < 2400 ? ch : 2399;
      const int row = ch / 10, cc = ch - row * 10;
      const u16* p = row < 192 ? wo + (size_t)(h * DH + row) * DM : w2 + (size_t)(h * 48 + row - 192) * DM;
      WS[i] = ld16(p + g * 80 + cc * 8);
    }
    XS = ld16(x + (tid < 320 ? tid : 319) * 8);
  }
  const u16* gp = gw + 10 * lane;
  const u32x4 ga = *reinterpret_cast<const u32x4_a4*>(gp);
  const u32 gb = *reinterpret_cast<const u32*>(gp + 8);
  const u64 ep_v = __hip_atomic_load((gu64*)(ws + WS_EPOCH), RLX_AGENT);
  PIN();
#pragma unroll
  for (int k = 0; k < 24; ++k) acc[k] = rsum8(acc[k]);
  if (r == 0) {
#pragma unroll
    for (int j = 0; j < 3; ++j) {
      *reinterpret_cast<f32x4*>(&red[wave * DH + j * 64 + c * 8]) = f32x4{acc[j * 8], acc[j * 8 + 1], acc[j * 8 + 2], acc[j * 8 + 3]};
      *reinterpret_cast<f32x4*>(&red[wave * DH + j * 64 + c * 8 + 4]) = f32x4{acc[j * 8 + 4], acc[j * 8 + 5], acc[j * 8 + 6], acc[j * 8 + 7]};
    }
  }
  WG_BARRIER();
  if (tid < DH) {
    float s = 0.f;
#pragma unroll
    for (int w = 0; w < 8; ++w) s += red[w * DH + tid];
    qs[tid] = (u16)cvtpk(s, s);
  }
  WG_BARRIER();
  u32x4 q[3];
#pragma unroll
  for (int j = 0; j < 3; ++j) q[j] = *reinterpret_cast<const u32x4*>(&qs[j * 64 + c * 8]);
  constexpr float C2 = 0.07216878364870322f * 1.4426950408889634f;
  float m = -INFINITY, l = 0.f;
#pragma unroll
  for (int k = 0; k < 24; ++k) acc[k] = 0.f;

#define TILE(T_NEXT, HAS_NEXT) do { \
    float s[4]; \
    _Pragma("unroll") for (int i = 0; i < 4; ++i) { float a0 = 0.f, a1 = 0.f, a2 = 0.f; \
      _Pragma("unroll") for (int e = 0; e < 4; ++e) { a0 = dot2(K[i][0][e], q[0][e], a0); a1 = dot2(K[i][1][e], q[1][e], a1); a2 = dot2(K[i][2][e], q[2][e], a2); } \
      s[i] = a0 + a1 + a2; } \
    asm volatile("" : "+v"(s[0]), "+v"(s[1]), "+v"(s[2]), "+v"(s[3])); \
    PIN(); \
    if (HAS_NEXT) { LD_TILE(K, rk, T_NEXT); } \
    PIN(); \
    _Pragma("unroll") for (int i = 0; i < 4; ++i) s[i] = csum8(s[i]) * C2; \
    const float mn = fmaxf(fmaxf(fmaxf(s[0], s[1]), fmaxf(s[2], s[3])), m); \
    const float al = __builtin_amdgcn_exp2f(m - mn); m = mn; \
    float p[4]; \
    _Pragma("unroll") for (int i = 0; i < 4; ++i) p[i] = __builtin_amdgcn_exp2f(s[i] - mn); \
    l = fmaf(l, al, (p[0] + p[1]) + (p[2] + p[3])); \
    _Pragma("unroll") for (int k = 0; k < 24; ++k) acc[k] *= al; \
    _Pragma("unroll") for (int i = 0; i < 4; ++i) _Pragma("unroll") for (int j = 0; j < 3; ++j) _Pragma("unroll") for (int e = 0; e < 4; ++e) { \
      acc[j * 8 + 2 * e] = fmaf(p[i], bf_lo(V[i][j][e]), acc[j * 8 + 2 * e]); acc[j * 8 + 2 * e + 1] = fmaf(p[i], bf_hi(V[i][j][e]), acc[j * 8 + 2 * e + 1]); } \
    PIN_ACC(acc); \
    asm volatile("" : "+v"(m), "+v"(l)); \
    PIN(); \
    if (HAS_NEXT) { LD_TILE(V, rv, T_NEXT); } \
    PIN(); \
  } while (0)

  TILE(1, true);

  const u64 tag = (((u64)(u32)__builtin_amdgcn_readfirstlane((u32)(ep_v >> 32)) << 32) | (u32)__builtin_amdgcn_readfirstlane((u32)ep_v)) + 1ull;
  u32x4 W1[10];
  {
    if (tid < 320) {
      const int bb = tid / 80, k0 = (tid - bb * 80) * 8;
#pragma unroll
      for (int e = 0; e < 4; ++e) { xs[(k0 + 2 * e) * 4 + bb] = bf_lo(XS[e]); xs[(k0 + 2 * e + 1) * 4 + bb] = bf_hi(XS[e]); }
    }
#pragma unroll
    for (int i = 0; i < 5; ++i) {
      const int ch = tid + i * NTHR;
      if (ch < 2400) {
        const int row = ch / 10, cc = ch - row * 10;
#pragma unroll
        for (int e = 0; e < 4; ++e) { wt[(cc * 8 + 2 * e) * WTS + row] = (u16)WS[i][e]; wt[(cc * 8 + 2 * e + 1) * WTS + row] = (u16)(WS[i][e] >> 16); }
      }
    }
    if (tid < 160) *reinterpret_cast<u32x4*>(&wt[(tid >> 1) * WTS + 240 + (tid & 1) * 8]) = u32x4{0u, 0u, 0u, 0u};
    const u32 gv[5] = {ga[0], ga[1], ga[2], ga[3], gb};
    float gwf[10];
#pragma unroll
    for (int i = 0; i < 10; ++i) gwf[i] = (i & 1) ? bf_hi(gv[i >> 1]) : bf_lo(gv[i >> 1]);
    PIN();
    const rsrc_t rw1 = mk_rsrc(w1 + h * 48, (DM * FF - h * 48) * 2);
    const int vo = (10 * r * FF + (c < 6 ? c : 5) * 8) * 2, so = 80 * wave * FF * 2;
#pragma unroll
    for (int i = 0; i < 10; ++i) W1[i] = ld_w(rw1, vo, so + i * FF * 2);
    PIN();
    WG_BARRIER();
    if (wave < 4) {
      float s = 0.f;
#pragma unroll
      for (int i = 0; i < 10; ++i) s += rbf(xs[(10 * lane + i) * 4 + wave] * gwf[i]);
      s = rsum8(csum8(s));
      const float z = rbf(s), gg = rbf(1.f / (1.f + __expf(-z)));
      if (lane == 0) { gs[wave] = gg; gs[4 + wave] = rbf(1.f - gg); }
    }
    PIN();
  }
  TILE(2, true);
  {
    float fa[32];
#pragma unroll
    for (int k = 0; k < 32; ++k) fa[k] = 0.f;
#pragma unroll
    for (int i = 0; i < 10; ++i) {
      const f32x4 xv = *reinterpret_cast<const f32x4*>(&xs[(80 * wave + 10 * r + i) * 4]);
#pragma unroll
      for (int e = 0; e < 4; ++e)
#pragma unroll
        for (int bb = 0; bb < 4; ++bb) {
          fa[bb * 8 + 2 * e] = fmaf(xv[bb], bf_lo(W1[i][e]), fa[bb * 8 + 2 * e]); fa[bb * 8 + 2 * e + 1] = fmaf(xv[bb], bf_hi(W1[i][e]), fa[bb * 8 + 2 * e + 1]); }
    }
#pragma unroll
    for (int k = 0; k < 32; ++k) fa[k] = rsum8(fa[k]);
    if (r == 0 && c < 6) {
#pragma unroll
      for (int bb = 0; bb < 4; ++bb) {
        *reinterpret_cast<f32x4*>(&red[wave * DH + bb * 48 + c * 8]) = f32x4{fa[bb * 8], fa[bb * 8 + 1], fa[bb * 8 + 2], fa[bb * 8 + 3]};
        *reinterpret_cast<f32x4*>(&red[wave * DH + bb * 48 + c * 8 + 4]) = f32x4{fa[bb * 8 + 4], fa[bb * 8 + 5], fa[bb * 8 + 6], fa[bb * 8 + 7]};
      }
    }
    WG_BARRIER();
    if (tid < 192) {
      float s = 0.f;
#pragma unroll
      for (int w = 0; w < 8; ++w) s += red[w * DH + tid];
      const float hv = rbf(s), a = hv / (1.f + __expf(-hv));
      const int bb = tid / 48;
      hs[bb * 64 + tid - bb * 48] = (u16)cvtpk(a, a);
    } else if (tid < 256) hs[((tid - 192) >> 4) * 64 + 48 + (tid & 15)] = 0;
    WG_BARRIER();
    if (wave == 0) {
      const f32x4 omg = *reinterpret_cast<const f32x4*>(&gs[4]);
#pragma unroll
      for (int t = 0; t < 5; ++t) {
        f32x4 a = {0.f, 0.f, 0.f, 0.f};
#pragma unroll
        for (int s = 0; s < 2; ++s) {
          const u32x4 af = *reinterpret_cast<const u32x4*>(&hs[(lane & 3) * 64 + 32 * s + 8 * (lane >> 4)]);
          const u32x4 bf = *reinterpret_cast<const u32x4*>(&wt[(16 * t + (lane & 15)) * WTS + 192 + 32 * s + 8 * (lane >> 4)]);
          a = __builtin_amdgcn_mfma_f32_16x16x32_bf16(__builtin_bit_cast(bf16x8, af), __builtin_bit_cast(bf16x8, bf), a, 0, 0, 0);
        }
        if (lane < 16) *reinterpret_cast<f32x4*>(&fb[(16 * t + lane) * 4]) = a * omg;
      }
    }
    PIN();
  }

#pragma unroll 1
  for (int t = 2; t < NT - 1; ++t) TILE(t + 1, true);
  TILE(0, false);
#undef TILE
#undef LD_TILE

  {
    const float M = rmax8(m), sc = __builtin_amdgcn_exp2f(m - M);
    l = rsum8(l * sc);
#pragma unroll
    for (int k = 0; k < 24; ++k) acc[k] = rsum8(acc[k] * sc);
    if (r == 0) {
#pragma unroll
      for (int j = 0; j < 3; ++j) {
        *reinterpret_cast<f32x4*>(&wp[wave * 196 + j * 64 + c * 8]) = f32x4{acc[j * 8], acc[j * 8 + 1], acc[j * 8 + 2], acc[j * 8 + 3]};
        *reinterpret_cast<f32x4*>(&wp[wave * 196 + j * 64 + c * 8 + 4]) = f32x4{acc[j * 8 + 4], acc[j * 8 + 5], acc[j * 8 + 6], acc[j * 8 + 7]};
      }
      if (c == 0) { wp[wave * 196 + 192] = M; wp[wave * 196 + 193] = l; }
    }
  }
  WG_BARRIER();
  const bool reducer = (h == 4 * g);
  const rsrc_t rs = mk_rsrc(ws, WS_END);
  if (wave == 0) {
    float M = -INFINITY;
#pragma unroll
    for (int w = 0; w < 8; ++w) M = fmaxf(M, wp[w * 196 + 192]);
    f32x4 o4 = {0.f, 0.f, 0.f, 0.f}; float L = 0.f;
    const int l4 = (lane < 48 ? lane : 47) * 4;
#pragma unroll
    for (int w = 0; w < 8; ++w) {
      const float wt_ = __builtin_amdgcn_exp2f(wp[w * 196 + 192] - M);
      L = fmaf(wt_, wp[w * 196 + 193], L);
      o4 += *reinterpret_cast<const f32x4*>(&wp[w * 196 + l4]) * wt_;
    }
    if (lane == 48) o4 = f32x4{M, L, 0.f, 0.f};
    if (lane < 49) __builtin_amdgcn_raw_buffer_store_b128(__builtin_bit_cast(u32x4, o4), rs, WS_P1 + (h * 8 + mem) * 1024 + lane * 16, 0, 16);
    asm volatile("s_waitcnt vmcnt(0)" ::: "memory");
    if (lane == 0) __hip_atomic_store((gu64*)(ws + WS_F1 + (h * 8 + mem) * 128), tag, RLX_AGENT);
    u32x4 bfr[5][6];
#pragma unroll
    for (int t = 0; t < 5; ++t)
#pragma unroll
      for (int s = 0; s < 6; ++s) bfr[t][s] = *reinterpret_cast<const u32x4*>(&wt[(16 * t + (lane & 15)) * WTS + 32 * s + 8 * (lane >> 4)]);
    const f32x4 gg = *reinterpret_cast<const f32x4*>(&gs[0]);
    f32x4 fbv[5];
#pragma unroll
    for (int t = 0; t < 5; ++t) fbv[t] = *reinterpret_cast<const f32x4*>(&fb[(16 * t + (lane & 15)) * 4]);
    PIN();
    wait_flags(ws + WS_F1 + h * 8 * 128, 8, lane, tag);
    u32x4 pr[8];
    const int lo = (lane < 49 ? lane : 48) * 16;
#pragma unroll
    for (int mm = 0; mm < 8; ++mm) pr[mm] = __builtin_amdgcn_raw_buffer_load_b128(rs, WS_P1 + (h * 8 + mm) * 1024 + lo, 0, 16);
#pragma unroll
    for (int bb = 0; bb < 4; ++bb) {
      const f32x4 v0 = __builtin_bit_cast(f32x4, pr[2 * bb]), v1 = __builtin_bit_cast(f32x4, pr[2 * bb + 1]);
      const float M0 = __builtin_bit_cast(float, __builtin_amdgcn_readlane(pr[2 * bb][0], 48)), L0 = __builtin_bit_cast(float, __builtin_amdgcn_readlane(pr[2 * bb][1], 48));
      const float M1 = __builtin_bit_cast(float, __builtin_amdgcn_readlane(pr[2 * bb + 1][0], 48)), L1 = __builtin_bit_cast(float, __builtin_amdgcn_readlane(pr[2 * bb + 1][1], 48));
      const float Mb = fmaxf(M0, M1), w0 = __builtin_amdgcn_exp2f(M0 - Mb), w1_ = __builtin_amdgcn_exp2f(M1 - Mb);
      const float inv = 1.f / fmaf(w0, L0, w1_ * L1);
      const f32x4 o = (v0 * w0 + v1 * w1_) * inv;
      if (lane < 48) *reinterpret_cast<u32x2*>(&us[bb * DH + 4 * lane]) = u32x2{cvtpk(o[0], o[1]), cvtpk(o[2], o[3])};
    }
    u32x4 af[6];
#pragma unroll
    for (int s = 0; s < 6; ++s) af[s] = *reinterpret_cast<const u32x4*>(&us[(lane & 3) * DH + 32 * s + 8 * (lane >> 4)]);
#pragma unroll
    for (int t = 0; t < 5; ++t) {
      f32x4 a = {0.f, 0.f, 0.f, 0.f};
#pragma unroll
      for (int s = 0; s < 6; ++s) a = __builtin_amdgcn_mfma_f32_16x16x32_bf16(__builtin_bit_cast(bf16x8, af[s]), __builtin_bit_cast(bf16x8, bfr[t][s]), a, 0, 0, 0);
      const f32x4 res = a * gg + fbv[t];
      if (lane < 16) __builtin_amdgcn_raw_buffer_store_b128(__builtin_bit_cast(u32x4, res), rs, WS_P2 + (g * 32 + h) * 1280 + (16 * t + lane) * 16, 0, 16);
    }
    asm volatile("s_waitcnt vmcnt(0)" ::: "memory");
    if (lane == 0) __hip_atomic_store((gu64*)(ws + WS_F2 + (g * 32 + h) * 128), tag, RLX_AGENT);
  }
  if (!reducer) return;
  {
    wait_flags(ws + WS_F2 + (g * 32 + 4 * wave) * 128, 4, lane, tag);
    u32x4 pv[5];
#pragma unroll
    for (int i = 0; i < 5; ++i) pv[i] = __builtin_amdgcn_raw_buffer_load_b128(rs, WS_P2 + (g * 32 + 4 * wave + (lane >> 4)) * 1280 + (16 * i + (lane & 15)) * 16, 0, 16);
#pragma unroll
    for (int i = 0; i < 5; ++i) {
      const f32x4 v = __builtin_bit_cast(f32x4, pv[i]);
      const f32x4 s = {xrow16_sum(v[0]), xrow16_sum(v[1]), xrow16_sum(v[2]), xrow16_sum(v[3])};
      if (lane < 16) *reinterpret_cast<f32x4*>(&red[wave * 320 + (16 * i + lane) * 4]) = s;
    }
    WG_BARRIER();
    if (tid < 320) {
      const int bb = tid / 80, n = tid - bb * 80;
      float s = 0.f;
#pragma unroll
      for (int w = 0; w < 8; ++w) s += red[w * 320 + n * 4 + bb];
      out[bb * DM + g * 80 + n] = (u16)cvtpk(s, s);
    }
    if (g == 0 && tid == 0) __hip_atomic_store((gu64*)(ws + WS_EPOCH), tag, RLX_AGENT);
  }
}

extern "C" void kernel_launch(void* const* d_in, const int* in_sizes, int n_in, void* d_out, int out_size, void* d_ws, size_t ws_size,
                              hipStream_t stream) {
  (void)in_sizes; (void)n_in; (void)out_size; (void)ws_size;
  fused_decode<<<256, NTHR, 0, stream>>>((const u16*)d_in[0], (const u16*)d_in[1], (const u16*)d_in[2], (const u16*)d_in[3], (const u16*)d_in[4],
                                         (const u16*)d_in[6], (const u16*)d_in[7], (const u16*)d_in[8], (u16*)d_out, (unsigned char*)d_ws);
}
```

```cpp
#include <hip/hip_runtime.h>
#include <hip/hip_bf16.h>
#include <stdint.h>

typedef unsigned short u16;
typedef unsigned int u32;
typedef unsigned long long u64;
using u32x4 = __attribute__((ext_vector_type(4))) unsigned;
using u32x2 = __attribute__((ext_vector_type(2))) unsigned;
using f32x4 = __attribute__((ext_vector_type(4))) float;
using bf16x2 = __attribute__((ext_vector_type(2))) __bf16;
using bf16x8 = __attribute__((ext_vector_type(8))) __bf16;
typedef u32x4 u32x4_a4 __attribute__((aligned(4)));
typedef __attribute__((address_space(1))) u64 gu64;

#define KV_NT 1

constexpr int DM = 640, NH = 32, SK = 4096, DH = 192, FF = 1536, QD = NH * DH;
constexpr int NTHR = 512;
constexpr int NT = 8;
constexpr int WTS = 264;
constexpr int WS_EPOCH = 0;
constexpr int WS_F1 = 4096;
constexpr int WS_F2 = WS_F1 + 256 * 128;
constexpr int WS_P1 = WS_F2 + 256 * 128;
constexpr int WS_P2 = WS_P1 + 256 * 1024;
constexpr int WS_Q = WS_P2 + 256 * 1280;
constexpr int WS_END = WS_Q + 32 * 4 * 192 * 8;
constexpr unsigned SPIN_CAP = 1u << 20;

#define RLX_AGENT __ATOMIC_RELAXED, __HIP_MEMORY_SCOPE_AGENT
#define WG_BARRIER() asm volatile("s_waitcnt lgkmcnt(0)\n\ts_barrier" ::: "memory")
#define PIN() do { asm volatile("" ::: "memory"); __builtin_amdgcn_sched_barrier(0); } while (0)

#define PIN_ACC(a) asm volatile("" : "+v"(a[0]), "+v"(a[1]), "+v"(a[2]), "+v"(a[3]), "+v"(a[4]), "+v"(a[5]), "+v"(a[6]), "+v"(a[7]), \
    "+v"(a[8]), "+v"(a[9]), "+v"(a[10]), "+v"(a[11]), "+v"(a[12]), "+v"(a[13]), "+v"(a[14]), "+v"(a[15]), \
    "+v"(a[16]), "+v"(a[17]), "+v"(a[18]), "+v"(a[19]), "+v"(a[20]), "+v"(a[21]), "+v"(a[22]), "+v"(a[23]) :: "memory")

__device__ __forceinline__ float dot2(unsigned k, unsigned q, float acc) {
  return __builtin_amdgcn_fdot2_f32_bf16(__builtin_bit_cast(bf16x2, k), __builtin_bit_cast(bf16x2, q), acc, false);
}
__device__ __forceinline__ unsigned cvtpk(float lo, float hi) {
  unsigned r; asm("v_cvt_pk_bf16_f32 %0, %1, %2" : "=v"(r) : "v"(lo), "v"(hi)); return r;
}
__device__ __forceinline__ float bf_lo(unsigned w) { return __builtin_bit_cast(float, w << 16); }
__device__ __forceinline__ float bf_hi(unsigned w) { return __builtin_bit_cast(float, w & 0xffff0000u); }
__device__ __forceinline__ float rbf(float x) { return bf_lo(cvtpk(x, x)); }
template <int CTRL> __device__ __forceinline__ float dpp(float x) {
  return __builtin_bit_cast(float, __builtin_amdgcn_mov_dpp(__builtin_bit_cast(int, x), CTRL, 0xf, 0xf, true));
}
constexpr int XOR1 = 0xB1, XOR2 = 0x4E, XOR7 = 0x141, ROR8 = 0x128;
__device__ __forceinline__ float xrow16_max(float x) {
  auto s = __builtin_amdgcn_permlane16_swap(__float_as_uint(x), __float_as_uint(x), false, false);
  x = fmaxf(__uint_as_float(s[0]), __uint_as_float(s[1]));
  auto t = __builtin_amdgcn_permlane32_swap(__float_as_uint(x), __float_as_uint(x), false, false);
  return fmaxf(__uint_as_float(t[0]), __uint_as_float(t[1]));
}
__device__ __forceinline__ float xrow16_sum(float x) {
  auto s = __builtin_amdgcn_permlane16_swap(__float_as_uint(x), __float_as_uint(x), false, false);
  x = __uint_as_float(s[0]) + __uint_as_float(s[1]);
  auto t = __builtin_amdgcn_permlane32_swap(__float_as_uint(x), __float_as_uint(x), false, false);
  return __uint_as_float(t[0]) + __uint_as_float(t[1]);
}
__device__ __forceinline__ float rsum8(float x) { x += dpp<ROR8>(x); return xrow16_sum(x); }
__device__ __forceinline__ float rmax8(float x) { x = fmaxf(x, dpp<ROR8>(x)); return xrow16_max(x); }
__device__ __forceinline__ float csum8(float x) { x += dpp<XOR1>(x); x += dpp<XOR2>(x); x += dpp<XOR7>(x); return x; }
typedef __amdgpu_buffer_rsrc_t rsrc_t;
__device__ __forceinline__ rsrc_t mk_rsrc(const void* p, unsigned bytes) { return __builtin_amdgcn_make_buffer_rsrc((void*)p, 0, bytes, 0x00020000); }
__device__ __forceinline__ u32x4 ld_kv(rsrc_t rs, int voff, int soff) { return __builtin_amdgcn_raw_buffer_load_b128(rs, voff, soff, KV_NT ? 2 : 0); }
__device__ __forceinline__ u32x4 ld_w(rsrc_t rs, int voff, int soff) { return __builtin_amdgcn_raw_buffer_load_b128(rs, voff, soff, 0); }
__device__ __forceinline__ u32x4 ld16(const u16* p) { return *reinterpret_cast<const u32x4*>(p); }
__device__ __forceinline__ void wait_flags(const unsigned char* base, int n, int lane, u64 tag) {
  gu64* f = (gu64*)(base + (size_t)(lane < n ? lane : 0) * 128);
  for (unsigned spins = 0; spins < SPIN_CAP; ++spins) {
    const u64 v = __hip_atomic_load(f, RLX_AGENT);
    if (__all(v == tag)) break;
    __builtin_amdgcn_s_sleep(1);
  }
  asm volatile("" ::: "memory");
}

#define STAMP(i) do {} while (0)
#define STAMP_FLUSH() do {} while (0)

__global__ void __launch_bounds__(NTHR, 2)
fused_decode(const u16* __restrict__ x, const u16* __restrict__ kc, const u16* __restrict__ vc, const u16* __restrict__ wq,
             const u16* __restrict__ wo, const u16* __restrict__ w1, const u16* __restrict__ w2, const u16* __restrict__ gw,
             u16* __restrict__ out, unsigned char* __restrict__ ws) {
  __shared__ __attribute__((aligned(16))) float xs[DM * 4];
  __shared__ __attribute__((aligned(16))) u16 wt[80 * WTS];
  __shared__ __attribute__((aligned(16))) float red[8 * 320];
  __shared__ __attribute__((aligned(16))) float wp[8 * 196];
  __shared__ __attribute__((aligned(16))) u16 qs[8 * DH];
  __shared__ __attribute__((aligned(16))) u16 hs[4 * 64];
  __shared__ __attribute__((aligned(16))) u16 us[4 * DH];
  __shared__ __attribute__((aligned(16))) float fb[320];
  __shared__ __attribute__((aligned(16))) float gs[8];

  const int bid = blockIdx.x, tid = threadIdx.x, lane = tid & 63;
  const int wave = __builtin_amdgcn_readfirstlane(tid >> 6);
  const int r = lane >> 3, c = lane & 7;
  const int h = (bid & 7) * 4 + (bid >> 6), mem = (bid >> 3) & 7, b = mem >> 1, half = mem & 1, g = mem;

  STAMP(0);
  u32x4 xa[4]; u32 xb[4];
#pragma unroll
  for (int bb = 0; bb < 4; ++bb) {
    const u16* xp = x + bb * DM + 80 * wave + 10 * r;
    xa[bb] = *reinterpret_cast<const u32x4_a4*>(xp);
    xb[bb] = *reinterpret_cast<const u32*>(xp + 8);
  }
  const rsrc_t rwq = mk_rsrc(wq + h * DH + 24 * mem, (DM * QD - h * DH - 24 * mem) * 2);
  u32x2 W[10];
  {
    const int vo = (10 * r * QD + (c < 6 ? c : 5) * 4) * 2, so = 80 * wave * QD * 2;
#pragma unroll
    for (int i = 0; i < 10; ++i) W[i] = __builtin_amdgcn_raw_buffer_load_b64(rwq, vo, so + i * QD * 2, 0);
  }
  PIN();
  const size_t kvoff = ((size_t)(b * NH + h) * SK + half * 2048) * DH;
  const rsrc_t rk = mk_rsrc(kc + kvoff, 2048 * DH * 2), rv = mk_rsrc(vc + kvoff, 2048 * DH * 2);
  const int kvo = r * (DH * 2) + c * 16, kso = wave * (32 * DH * 2);
  u32x4 K[4][3], V[4][3];
#define LD_ROWS(T, RS, TI, I0, I1) _Pragma("unroll") for (int i = (I0); i < (I1); ++i) _Pragma("unroll") for (int j = 0; j < 3; ++j) \
    T[i][j] = ld_kv(RS, kvo + (i & 1) * 3072 + j * 128, kso + (TI) * (256 * DH * 2) + (i >> 1) * 6144)
#define LD_TILE(T, RS, TI) LD_ROWS(T, RS, TI, 0, 4)
  LD_TILE(K, rk, 0);
  PIN();
  u64 ep;
  asm volatile("s_load_dwordx2 %0, %1, 0x0\n\ts_waitcnt lgkmcnt(0)" : "=s"(ep) : "s"(ws) : "memory");
  PIN();
  const u64 tag = ep + 1ull;
  const rsrc_t rs = mk_rsrc(ws, WS_END);
  float acc[24];
  {
    float qa[16];
#pragma unroll
    for (int k = 0; k < 16; ++k) qa[k] = 0.f;
#pragma unroll
    for (int i = 0; i < 10; ++i) {
      const float w0 = bf_lo(W[i][0]), w1_ = bf_hi(W[i][0]), w2_ = bf_lo(W[i][1]), w3 = bf_hi(W[i][1]);
#pragma unroll
      for (int bb = 0; bb < 4; ++bb) {
        const u32 xw = i < 8 ? xa[bb][i >> 1] : xb[bb];
        const float xv = (i & 1) ? bf_hi(xw) : bf_lo(xw);
        qa[bb * 4] = fmaf(xv, w0, qa[bb * 4]); qa[bb * 4 + 1] = fmaf(xv, w1_, qa[bb * 4 + 1]);
        qa[bb * 4 + 2] = fmaf(xv, w2_, qa[bb * 4 + 2]); qa[bb * 4 + 3] = fmaf(xv, w3, qa[bb * 4 + 3]);
      }
    }
#pragma unroll
    for (int k = 0; k < 16; ++k) qa[k] = rsum8(qa[k]);
    if (r == 0 && c < 6) {
#pragma unroll
      for (int bb = 0; bb < 4; ++bb)
        *reinterpret_cast<f32x4*>(&red[wave * 96 + bb * 24 + c * 4]) = f32x4{qa[bb * 4], qa[bb * 4 + 1], qa[bb * 4 + 2], qa[bb * 4 + 3]};
    }
  }
  STAMP(1);
  WG_BARRIER();
  if (tid < 96) {
    float s = 0.f;
#pragma unroll
    for (int w = 0; w < 8; ++w) s += red[w * 96 + tid];
    const int bb = tid / 24, cc = tid - bb * 24;
    const u64 gr = (tag << 16) | (u64)(cvtpk(s, s) & 0xffffu);
    __hip_atomic_store((gu64*)(ws + WS_Q + ((h * 4 + bb) * DH + 24 * mem + cc) * 8), gr, RLX_AGENT);
  }
  PIN();
  LD_ROWS(V, rv, 0, 0, 2);
  PIN();
  u32x2 qg[3];
  const int qgo = WS_Q + (h * 4 + b) * DH * 8 + lane * 8;
#pragma unroll
  for (int j = 0; j < 3; ++j) qg[j] = __builtin_amdgcn_raw_buffer_load_b64(rs, qgo + j * 512, 0, 16);
  PIN();
  LD_ROWS(V, rv, 0, 2, 4);
  PIN();
  u32x4 WS[5], XS;
  {
#pragma unroll
    for (int i = 0; i < 5; ++i) {
      int ch = tid + i * NTHR; ch = ch < 2400 ? ch : 2399;
      const int row = ch / 10, cc = ch - row * 10;
      const u16* p = row < 192 ? wo + (size_t)(h * DH + row) * DM : w2 + (size_t)(h * 48 + row - 192) * DM;
      WS[i] = ld16(p + g * 80 + cc * 8);
    }
    XS = ld16(x + (tid < 320 ? tid : 319) * 8);
  }
  const u16* gp = gw + 10 * lane;
  const u32x4 ga = *reinterpret_cast<const u32x4_a4*>(gp);
  const u32 gb = *reinterpret_cast<const u32*>(gp + 8);
  PIN();
  {
    const u32 tlo = (u32)(tag << 16), thi = (u32)(tag >> 16);
#define QG_OK() __all(((qg[0][0] ^ tlo) >> 16 | (qg[1][0] ^ tlo) >> 16 | (qg[2][0] ^ tlo) >> 16 | (qg[0][1] ^ thi) | (qg[1][1] ^ thi) | (qg[2][1] ^ thi)) == 0u)
    if (!QG_OK()) {
      for (unsigned spins = 0; spins < SPIN_CAP; ++spins) {
#pragma unroll
        for (int j = 0; j < 3; ++j) qg[j] = __builtin_amdgcn_raw_buffer_load_b64(rs, qgo + j * 512, 0, 16);
        if (QG_OK()) break;
        __builtin_amdgcn_s_sleep(1);
      }
    }
#undef QG_OK
#pragma unroll
    for (int j = 0; j < 3; ++j) qs[wave * DH + j * 64 + lane] = (u16)qg[j][0];
  }
  asm volatile("s_waitcnt lgkmcnt(0)" ::: "memory");
  u32x4 q[3];
#pragma unroll
  for (int j = 0; j < 3; ++j) q[j] = *reinterpret_cast<const u32x4*>(&qs[wave * DH + j * 64 + c * 8]);
  STAMP(2);
  constexpr float C2 = 0.07216878364870322f * 1.4426950408889634f;
  float m = -INFINITY, l = 0.f;
#pragma unroll
  for (int k = 0; k < 24; ++k) acc[k] = 0.f;

#define TILE(T_NEXT, HAS_NEXT) do { \
    float s[4]; \
    _Pragma("unroll") for (int i = 0; i < 4; ++i) { float a0 = 0.f, a1 = 0.f, a2 = 0.f; \
      _Pragma("unroll") for (int e = 0; e < 4; ++e) { a0 = dot2(K[i][0][e], q[0][e], a0); a1 = dot2(K[i][1][e], q[1][e], a1); a2 = dot2(K[i][2][e], q[2][e], a2); } \
      s[i] = a0 + a1 + a2; } \
    asm volatile("" : "+v"(s[0]), "+v"(s[1]), "+v"(s[2]), "+v"(s[3])); \
    PIN(); \
    if (HAS_NEXT) { LD_TILE(K, rk, T_NEXT); } \
    PIN(); \
    _Pragma("unroll") for (int i = 0; i < 4; ++i) s[i] = csum8(s[i]) * C2; \
    const float mn = fmaxf(fmaxf(fmaxf(s[0], s[1]), fmaxf(s[2], s[3])), m); \
    const float al = __builtin_amdgcn_exp2f(m - mn); m = mn; \
    float p[4]; \
    _Pragma("unroll") for (int i = 0; i < 4; ++i) p[i] = __builtin_amdgcn_exp2f(s[i] - mn); \
    l = fmaf(l, al, (p[0] + p[1]) + (p[2] + p[3])); \
    _Pragma("unroll") for (int k = 0; k < 24; ++k) acc[k] *= al; \
    _Pragma("unroll") for (int i = 0; i < 4; ++i) _Pragma("unroll") for (int j = 0; j < 3; ++j) _Pragma("unroll") for (int e = 0; e < 4; ++e) { \
      acc[j * 8 + 2 * e] = fmaf(p[i], bf_lo(V[i][j][e]), acc[j * 8 + 2 * e]); acc[j * 8 + 2 * e + 1] = fmaf(p[i], bf_hi(V[i][j][e]), acc[j * 8 + 2 * e + 1]); } \
    PIN_ACC(acc); \
    asm volatile("" : "+v"(m), "+v"(l)); \
    PIN(); \
    if (HAS_NEXT) { LD_TILE(V, rv, T_NEXT); } \
    PIN(); \
  } while (0)

  TILE(1, true);
  STAMP(3);

  u32x4 W1[10];
  {
    if (tid < 320) {
      const int bb = tid / 80, k0 = (tid - bb * 80) * 8;
#pragma unroll
      for (int e = 0; e < 4; ++e) { xs[(k0 + 2 * e) * 4 + bb] = bf_lo(XS[e]); xs[(k0 + 2 * e + 1) * 4 + bb] = bf_hi(XS[e]); }
    }
#pragma unroll
    for (int i = 0; i < 5; ++i) {
      const int ch = tid + i * NTHR;
      if (ch < 2400) {
        const int row = ch / 10, cc = ch - row * 10;
#pragma unroll
        for (int e = 0; e < 4; ++e) { wt[(cc * 8 + 2 * e) * WTS + row] = (u16)WS[i][e]; wt[(cc * 8 + 2 * e + 1) * WTS + row] = (u16)(WS[i][e] >> 16); }
      }
    }
    if (tid < 160) *reinterpret_cast<u32x4*>(&wt[(tid >> 1) * WTS + 240 + (tid & 1) * 8]) = u32x4{0u, 0u, 0u, 0u};
    const u32 gv[5] = {ga[0], ga[1], ga[2], ga[3], gb};
    float gwf[10];
#pragma unroll
    for (int i = 0; i < 10; ++i) gwf[i] = (i & 1) ? bf_hi(gv[i >> 1]) : bf_lo(gv[i >> 1]);
    PIN();
    const rsrc_t rw1 = mk_rsrc(w1 + h * 48, (DM * FF - h * 48) * 2);
    const int vo = (10 * r * FF + (c < 6 ? c : 5) * 8) * 2, so = 80 * wave * FF * 2;
#pragma unroll
    for (int i = 0; i < 10; ++i) W1[i] = ld_w(rw1, vo, so + i * FF * 2);
    PIN();
    WG_BARRIER();
    if (wave < 4) {
      float s = 0.f;
#pragma unroll
      for (int i = 0; i < 10; ++i) s += rbf(xs[(10 * lane + i) * 4 + wave] * gwf[i]);
      s = rsum8(csum8(s));
      const float z = rbf(s), gg = rbf(1.f / (1.f + __expf(-z)));
      if (lane == 0) { gs[wave] = gg; gs[4 + wave] = rbf(1.f - gg); }
    }
    PIN();
  }
  TILE(2, true);
  {
    float fa[32];
#pragma unroll
    for (int k = 0; k < 32; ++k) fa[k] = 0.f;
#pragma unroll
    for (int i = 0; i < 10; ++i) {
      const f32x4 xv = *reinterpret_cast<const f32x4*>(&xs[(80 * wave + 10 * r + i) * 4]);
#pragma unroll
      for (int e = 0; e < 4; ++e)
#pragma unroll
        for (int bb = 0; bb < 4; ++bb) {
          fa[bb * 8 + 2 * e] = fmaf(xv[bb], bf_lo(W1[i][e]), fa[bb * 8 + 2 * e]); fa[bb * 8 + 2 * e + 1] = fmaf(xv[bb], bf_hi(W1[i][e]), fa[bb * 8 + 2 * e + 1]); }
    }
#pragma unroll
    for (int k = 0; k < 32; ++k) fa[k] = rsum8(fa[k]);
    if (r == 0 && c < 6) {
#pragma unroll
      for (int bb = 0; bb < 4; ++bb) {
        *reinterpret_cast<f32x4*>(&red[wave * DH + bb * 48 + c * 8]) = f32x4{fa[bb * 8], fa[bb * 8 + 1], fa[bb * 8 + 2], fa[bb * 8 + 3]};
        *reinterpret_cast<f32x4*>(&red[wave * DH + bb * 48 + c * 8 + 4]) = f32x4{fa[bb * 8 + 4], fa[bb * 8 + 5], fa[bb * 8 + 6], fa[bb * 8 + 7]};
      }
    }
    WG_BARRIER();
    if (tid < 192) {
      float s = 0.f;
#pragma unroll
      for (int w = 0; w < 8; ++w) s += red[w * DH + tid];
      const float hv = rbf(s), a = hv / (1.f + __expf(-hv));
      const int bb = tid / 48;
      hs[bb * 64 + tid - bb * 48] = (u16)cvtpk(a, a);
    } else if (tid < 256) hs[((tid - 192) >> 4) * 64 + 48 + (tid & 15)] = 0;
    WG_BARRIER();
    if (wave == 0) {
      const f32x4 omg = *reinterpret_cast<const f32x4*>(&gs[4]);
#pragma unroll
      for (int t = 0; t < 5; ++t) {
        f32x4 a = {0.f, 0.f, 0.f, 0.f};
#pragma unroll
        for (int s = 0; s < 2; ++s) {
          const u32x4 af = *reinterpret_cast<const u32x4*>(&hs[(lane & 3) * 64 + 32 * s + 8 * (lane >> 4)]);
          const u32x4 bf = *reinterpret_cast<const u32x4*>(&wt[(16 * t + (lane & 15)) * WTS + 192 + 32 * s + 8 * (lane >> 4)]);
          a = __builtin_amdgcn_mfma_f32_16x16x32_bf16(__builtin_bit_cast(bf16x8, af), __builtin_bit_cast(bf16x8, bf), a, 0, 0, 0);
        }
        if (lane < 16) *reinterpret_cast<f32x4*>(&fb[(16 * t + lane) * 4]) = a * omg;
      }
    }
    PIN();
  }

  STAMP(4);
#pragma unroll 1
  for (int t = 2; t < NT - 1; ++t) TILE(t + 1, true);
  TILE(0, false);
#undef TILE
#undef LD_TILE

  STAMP(5);
  {
    const float M = rmax8(m), sc = __builtin_amdgcn_exp2f(m - M);
    l = rsum8(l * sc);
#pragma unroll
    for (int k = 0; k < 24; ++k) acc[k] = rsum8(acc[k] * sc);
    if (r == 0) {
#pragma unroll
      for (int j = 0; j < 3; ++j) {
        *reinterpret_cast<f32x4*>(&wp[wave * 196 + j * 64 + c * 8]) = f32x4{acc[j * 8], acc[j * 8 + 1], acc[j * 8 + 2], acc[j * 8 + 3]};
        *reinterpret_cast<f32x4*>(&wp[wave * 196 + j * 64 + c * 8 + 4]) = f32x4{acc[j * 8 + 4], acc[j * 8 + 5], acc[j * 8 + 6], acc[j * 8 + 7]};
      }
      if (c == 0) { wp[wave * 196 + 192] = M; wp[wave * 196 + 193] = l; }
    }
  }
  WG_BARRIER();
  STAMP(6);
  const bool reducer = (h == 4 * g);
  if (wave == 0) {
    float M = -INFINITY;
#pragma unroll
    for (int w = 0; w < 8; ++w) M = fmaxf(M, wp[w * 196 + 192]);
    f32x4 o4 = {0.f, 0.f, 0.f, 0.f}; float L = 0.f;
    const int l4 = (lane < 48 ? lane : 47) * 4;
#pragma unroll
    for (int w = 0; w < 8; ++w) {
      const float wt_ = __builtin_amdgcn_exp2f(wp[w * 196 + 192] - M);
      L = fmaf(wt_, wp[w * 196 + 193], L);
      o4 += *reinterpret_cast<const f32x4*>(&wp[w * 196 + l4]) * wt_;
    }
    if (lane == 48) o4 = f32x4{M, L, 0.f, 0.f};
    if (lane < 49) __builtin_amdgcn_raw_buffer_store_b128(__builtin_bit_cast(u32x4, o4), rs, WS_P1 + (h * 8 + mem) * 1024 + lane * 16, 0, 16);
    asm volatile("s_waitcnt vmcnt(0)" ::: "memory");
    if (lane == 0) __hip_atomic_store((gu64*)(ws + WS_F1 + (h * 8 + mem) * 128), tag, RLX_AGENT);
    STAMP(7);
    u32x4 bfr[5][6];
#pragma unroll
    for (int t = 0; t < 5; ++t)
#pragma unroll
      for (int s = 0; s < 6; ++s) bfr[t][s] = *reinterpret_cast<const u32x4*>(&wt[(16 * t + (lane & 15)) * WTS + 32 * s + 8 * (lane >> 4)]);
    const f32x4 gg = *reinterpret_cast<const f32x4*>(&gs[0]);
    f32x4 fbv[5];
#pragma unroll
    for (int t = 0; t < 5; ++t) fbv[t] = *reinterpret_cast<const f32x4*>(&fb[(16 * t + (lane & 15)) * 4]);
    PIN();
    wait_flags(ws + WS_F1 + h * 8 * 128, 8, lane, tag);
    STAMP(8);
    u32x4 pr[8];
    const int lo = (lane < 49 ? lane : 48) * 16;
#pragma unroll
    for (int mm = 0; mm < 8; ++mm) pr[mm] = __builtin_amdgcn_raw_buffer_load_b128(rs, WS_P1 + (h * 8 + mm) * 1024 + lo, 0, 16);
#pragma unroll
    for (int bb = 0; bb < 4; ++bb) {
      const f32x4 v0 = __builtin_bit_cast(f32x4, pr[2 * bb]), v1 = __builtin_bit_cast(f32x4, pr[2 * bb + 1]);
      const float M0 = __builtin_bit_cast(float, __builtin_amdgcn_readlane(pr[2 * bb][0], 48)), L0 = __builtin_bit_cast(float, __builtin_amdgcn_readlane(pr[2 * bb][1], 48));
      const float M1 = __builtin_bit_cast(float, __builtin_amdgcn_readlane(pr[2 * bb + 1][0], 48)), L1 = __builtin_bit_cast(float, __builtin_amdgcn_readlane(pr[2 * bb + 1][1], 48));
      const float Mb = fmaxf(M0, M1), w0 = __builtin_amdgcn_exp2f(M0 - Mb), w1_ = __builtin_amdgcn_exp2f(M1 - Mb);
      const float inv = 1.f / fmaf(w0, L0, w1_ * L1);
      const f32x4 o = (v0 * w0 + v1 * w1_) * inv;
      if (lane < 48) *reinterpret_cast<u32x2*>(&us[bb * DH + 4 * lane]) = u32x2{cvtpk(o[0], o[1]), cvtpk(o[2], o[3])};
    }
    u32x4 af[6];
#pragma unroll
    for (int s = 0; s < 6; ++s) af[s] = *reinterpret_cast<const u32x4*>(&us[(lane & 3) * DH + 32 * s + 8 * (lane >> 4)]);
#pragma unroll
    for (int t = 0; t < 5; ++t) {
      f32x4 a = {0.f, 0.f, 0.f, 0.f};
#pragma unroll
      for (int s = 0; s < 6; ++s) a = __builtin_amdgcn_mfma_f32_16x16x32_bf16(__builtin_bit_cast(bf16x8, af[s]), __builtin_bit_cast(bf16x8, bfr[t][s]), a, 0, 0, 0);
      const f32x4 res = a * gg + fbv[t];
      if (lane < 16) __builtin_amdgcn_raw_buffer_store_b128(__builtin_bit_cast(u32x4, res), rs, WS_P2 + (g * 32 + h) * 1280 + (16 * t + lane) * 16, 0, 16);
    }
    asm volatile("s_waitcnt vmcnt(0)" ::: "memory");
    if (lane == 0) __hip_atomic_store((gu64*)(ws + WS_F2 + (g * 32 + h) * 128), tag, RLX_AGENT);
  }
  STAMP(9);
  if (!reducer) { STAMP_FLUSH(); return; }
  {
    wait_flags(ws + WS_F2 + (g * 32 + 4 * wave) * 128, 4, lane, tag);
    STAMP(10);
    u32x4 pv[5];
#pragma unroll
    for (int i = 0; i < 5; ++i) pv[i] = __builtin_amdgcn_raw_buffer_load_b128(rs, WS_P2 + (g * 32 + 4 * wave + (lane >> 4)) * 1280 + (16 * i + (lane & 15)) * 16, 0, 16);
#pragma unroll
    for (int i = 0; i < 5; ++i) {
      const f32x4 v = __builtin_bit_cast(f32x4, pv[i]);
      const f32x4 s = {xrow16_sum(v[0]), xrow16_sum(v[1]), xrow16_sum(v[2]), xrow16_sum(v[3])};
      if (lane < 16) *reinterpret_cast<f32x4*>(&red[wave * 320 + (16 * i + lane) * 4]) = s;
    }
    WG_BARRIER();
    if (tid < 320) {
      const int bb = tid / 80, n = tid - bb * 80;
      float s = 0.f;
#pragma unroll
      for (int w = 0; w < 8; ++w) s += red[w * 320 + n * 4 + bb];
      out[bb * DM + g * 80 + n] = (u16)cvtpk(s, s);
    }
    if (g == 0 && tid == 0) __hip_atomic_store((gu64*)(ws + WS_EPOCH), tag, RLX_AGENT);
    STAMP(11);
    STAMP_FLUSH();
  }
}

extern "C" void kernel_launch(void* const* d_in, const int* in_sizes, int n_in, void* d_out, int out_size, void* d_ws, size_t ws_size,
                              hipStream_t stream) {
  (void)in_sizes; (void)n_in; (void)out_size; (void)ws_size;
  fused_decode<<<256, NTHR, 0, stream>>>((const u16*)d_in[0], (const u16*)d_in[1], (const u16*)d_in[2], (const u16*)d_in[3], (const u16*)d_in[4],
                                         (const u16*)d_in[6], (const u16*)d_in[7], (const u16*)d_in[8], (u16*)d_out, (unsigned char*)d_ws);
}
```

```cpp
#include <hip/hip_runtime.h>
#include <hip/hip_bf16.h>
#include <stdint.h>

typedef unsigned short u16;
typedef unsigned int u32;
typedef unsigned long long u64;
using u32x4 = __attribute__((ext_vector_type(4))) unsigned;
using u32x2 = __attribute__((ext_vector_type(2))) unsigned;
using f32x4 = __attribute__((ext_vector_type(4))) float;
using bf16x2 = __attribute__((ext_vector_type(2))) __bf16;
using bf16x8 = __attribute__((ext_vector_type(8))) __bf16;
typedef u32x4 u32x4_a4 __attribute__((aligned(4)));
typedef __attribute__((address_space(1))) u64 gu64;


constexpr int DM = 640, NH = 32, SK = 4096, DH = 192, FF = 1536, QD = NH * DH;
constexpr int NTHR = 512;
constexpr int NT = 8;
constexpr int WTS = 264;
constexpr int WS_EPOCH = 0;
constexpr int WS_P1 = 4096;
constexpr int WS_P2 = WS_P1 + 256 * 2048;
constexpr int WS_Q = WS_P2 + 256 * 2560;
constexpr int WS_END = WS_Q + 32 * 4 * 192 * 8;
constexpr unsigned SPIN_CAP = 1u << 20;
constexpr int KV_AUX = 2;
constexpr int K0_FIRST = 2;
constexpr int POLL_SLEEP = 2;
constexpr int PACE = 720, PACE0 = 100;

#define RLX_AGENT __ATOMIC_RELAXED, __HIP_MEMORY_SCOPE_AGENT
#define WG_BARRIER() asm volatile("s_waitcnt lgkmcnt(0)\n\ts_barrier" ::: "memory")
#define PIN() do { asm volatile("" ::: "memory"); __builtin_amdgcn_sched_barrier(0); } while (0)

#define PIN_ACC(a) asm volatile("" : "+v"(a[0]), "+v"(a[1]), "+v"(a[2]), "+v"(a[3]), "+v"(a[4]), "+v"(a[5]), "+v"(a[6]), "+v"(a[7]), \
    "+v"(a[8]), "+v"(a[9]), "+v"(a[10]), "+v"(a[11]), "+v"(a[12]), "+v"(a[13]), "+v"(a[14]), "+v"(a[15]), \
    "+v"(a[16]), "+v"(a[17]), "+v"(a[18]), "+v"(a[19]), "+v"(a[20]), "+v"(a[21]), "+v"(a[22]), "+v"(a[23]) :: "memory")

__device__ __forceinline__ float dot2(unsigned k, unsigned q, float acc) {
  return __builtin_amdgcn_fdot2_f32_bf16(__builtin_bit_cast(bf16x2, k), __builtin_bit_cast(bf16x2, q), acc, false);
}
__device__ __forceinline__ unsigned cvtpk(float lo, float hi) {
  unsigned r; asm("v_cvt_pk_bf16_f32 %0, %1, %2" : "=v"(r) : "v"(lo), "v"(hi)); return r;
}
__device__ __forceinline__ float bf_lo(unsigned w) { return __builtin_bit_cast(float, w << 16); }
__device__ __forceinline__ float bf_hi(unsigned w) { return __builtin_bit_cast(float, w & 0xffff0000u); }
__device__ __forceinline__ float rbf(float x) { return bf_lo(cvtpk(x, x)); }
template <int CTRL> __device__ __forceinline__ float dpp(float x) {
  return __builtin_bit_cast(float, __builtin_amdgcn_mov_dpp(__builtin_bit_cast(int, x), CTRL, 0xf, 0xf, true));
}
constexpr int XOR1 = 0xB1, XOR2 = 0x4E, XOR7 = 0x141, ROR8 = 0x128;
__device__ __forceinline__ float xrow16_max(float x) {
  auto s = __builtin_amdgcn_permlane16_swap(__float_as_uint(x), __float_as_uint(x), false, false);
  x = fmaxf(__uint_as_float(s[0]), __uint_as_float(s[1]));
  auto t = __builtin_amdgcn_permlane32_swap(__float_as_uint(x), __float_as_uint(x), false, false);
  return fmaxf(__uint_as_float(t[0]), __uint_as_float(t[1]));
}
__device__ __forceinline__ float xrow16_sum(float x) {
  auto s = __builtin_amdgcn_permlane16_swap(__float_as_uint(x), __float_as_uint(x), false, false);
  x = __uint_as_float(s[0]) + __uint_as_float(s[1]);
  auto t = __builtin_amdgcn_permlane32_swap(__float_as_uint(x), __float_as_uint(x), false, false);
  return __uint_as_float(t[0]) + __uint_as_float(t[1]);
}
__device__ __forceinline__ float rsum8(float x) { x += dpp<ROR8>(x); return xrow16_sum(x); }
__device__ __forceinline__ float rmax8(float x) { x = fmaxf(x, dpp<ROR8>(x)); return xrow16_max(x); }
__device__ __forceinline__ float csum8(float x) { x += dpp<XOR1>(x); x += dpp<XOR2>(x); x += dpp<XOR7>(x); return x; }
typedef __amdgpu_buffer_rsrc_t rsrc_t;
__device__ __forceinline__ rsrc_t mk_rsrc(const void* p, unsigned bytes) { return __builtin_amdgcn_make_buffer_rsrc((void*)p, 0, bytes, 0x00020000); }
template <int AUX> __device__ __forceinline__ u32x4 ld_kv(rsrc_t rs, int voff, int soff) { return __builtin_amdgcn_raw_buffer_load_b128(rs, voff, soff, AUX); }
__device__ __forceinline__ u32x4 ld_w(rsrc_t rs, int voff, int soff) { return __builtin_amdgcn_raw_buffer_load_b128(rs, voff, soff, 0); }
__device__ __forceinline__ u32x4 ld16(const u16* p) { return *reinterpret_cast<const u32x4*>(p); }
__device__ __forceinline__ u32 enc24(float x) { return (__float_as_uint(x) + 0x80u) >> 8; }
__device__ __forceinline__ float dec24(u32 w) { return __uint_as_float(w << 8); }


__global__ void __launch_bounds__(NTHR, 2)
fused_decode(const u16* __restrict__ x, const u16* __restrict__ kc, const u16* __restrict__ vc, const u16* __restrict__ wq,
             const u16* __restrict__ wo, const u16* __restrict__ w1, const u16* __restrict__ w2, const u16* __restrict__ gw,
             u16* __restrict__ out, unsigned char* __restrict__ ws) {
  __shared__ __attribute__((aligned(16))) float xs[DM * 4];
  __shared__ __attribute__((aligned(16))) u16 wt[80 * WTS];
  __shared__ __attribute__((aligned(16))) float red[8 * 320];
  __shared__ __attribute__((aligned(16))) float wp[8 * 196];
  __shared__ __attribute__((aligned(16))) u16 qs[8 * DH];
  __shared__ __attribute__((aligned(16))) u16 hs[4 * 64];
  __shared__ __attribute__((aligned(16))) u16 us[4 * DH];
  __shared__ __attribute__((aligned(16))) float fb[320];
  __shared__ __attribute__((aligned(16))) float gs[8];

  const int bid = blockIdx.x, tid = threadIdx.x, lane = tid & 63;
  const int wave = __builtin_amdgcn_readfirstlane(tid >> 6);
  const int r = lane >> 3, c = lane & 7;
  const int h = (bid & 7) * 4 + (bid >> 6), mem = (bid >> 3) & 7, b = mem >> 1, half = mem & 1, g = mem;
  const u64 t_start = __builtin_amdgcn_s_memrealtime();
  u32x4 xa[4]; u32 xb[4];
#pragma unroll
  for (int bb = 0; bb < 4; ++bb) {
    const u16* xp = x + bb * DM + 80 * wave + 10 * r;
    xa[bb] = *reinterpret_cast<const u32x4_a4*>(xp);
    xb[bb] = *reinterpret_cast<const u32*>(xp + 8);
  }
  const rsrc_t rwq = mk_rsrc(wq + h * DH + 24 * mem, (DM * QD - h * DH - 24 * mem) * 2);
  u32x2 W[10];
  {
    const int vo = (10 * r * QD + (c < 6 ? c : 5) * 4) * 2, so = 80 * wave * QD * 2;
#pragma unroll
    for (int i = 0; i < 10; ++i) W[i] = __builtin_amdgcn_raw_buffer_load_b64(rwq, vo, so + i * QD * 2, 0);
  }
  PIN();
  const size_t kvoff = ((size_t)(b * NH + h) * SK + half * 2048) * DH;
  const rsrc_t rk = mk_rsrc(kc + kvoff, 2048 * DH * 2), rv = mk_rsrc(vc + kvoff, 2048 * DH * 2);
  const int kvo = r * (DH * 2) + c * 16, kso = wave * (32 * DH * 2);
  u32x4 K[4][3], V[4][3];
#define LD_ROWS(T, RS, TI, I0, I1) _Pragma("unroll") for (int i = (I0); i < (I1); ++i) _Pragma("unroll") for (int j = 0; j < 3; ++j) \
    T[i][j] = ld_kv<KV_AUX>(RS, kvo + (i & 1) * 3072 + j * 128, kso + (TI) * (256 * DH * 2) + (i >> 1) * 6144)
#define LD_TILE(T, RS, TI) LD_ROWS(T, RS, TI, 0, 4)
  asm volatile("s_barrier" ::: "memory");
  PIN();
  LD_ROWS(K, rk, 0, 0, K0_FIRST);
  PIN();
  u64 ep;
  asm volatile("s_load_dwordx2 %0, %1, 0x0\n\ts_waitcnt lgkmcnt(0)" : "=s"(ep) : "s"(ws) : "memory");
  PIN();
  const u64 tag = ep + 1ull;
  const rsrc_t rs = mk_rsrc(ws, WS_END);
  float acc[24];
  {
    float qa[16];
#pragma unroll
    for (int k = 0; k < 16; ++k) qa[k] = 0.f;
#pragma unroll
    for (int i = 0; i < 10; ++i) {
      const float w0 = bf_lo(W[i][0]), w1_ = bf_hi(W[i][0]), w2_ = bf_lo(W[i][1]), w3 = bf_hi(W[i][1]);
#pragma unroll
      for (int bb = 0; bb < 4; ++bb) {
        const u32 xw = i < 8 ? xa[bb][i >> 1] : xb[bb];
        const float xv = (i & 1) ? bf_hi(xw) : bf_lo(xw);
        qa[bb * 4] = fmaf(xv, w0, qa[bb * 4]); qa[bb * 4 + 1] = fmaf(xv, w1_, qa[bb * 4 + 1]);
        qa[bb * 4 + 2] = fmaf(xv, w2_, qa[bb * 4 + 2]); qa[bb * 4 + 3] = fmaf(xv, w3, qa[bb * 4 + 3]);
      }
    }
#pragma unroll
    for (int k = 0; k < 16; ++k) qa[k] = rsum8(qa[k]);
    if (r == 0 && c < 6) {
#pragma unroll
      for (int bb = 0; bb < 4; ++bb)
        *reinterpret_cast<f32x4*>(&red[wave * 96 + bb * 24 + c * 4]) = f32x4{qa[bb * 4], qa[bb * 4 + 1], qa[bb * 4 + 2], qa[bb * 4 + 3]};
    }
  }
  WG_BARRIER();
  if (tid < 96) {
    float s = 0.f;
#pragma unroll
    for (int w = 0; w < 8; ++w) s += red[w * 96 + tid];
    const int bb = tid / 24, cc = tid - bb * 24;
    const u64 gr = (tag << 16) | (u64)(cvtpk(s, s) & 0xffffu);
    __hip_atomic_store((gu64*)(ws + WS_Q + ((h * 4 + bb) * DH + 24 * mem + cc) * 8), gr, RLX_AGENT);
  }
  PIN();
  LD_ROWS(K, rk, 0, K0_FIRST, 4);
  LD_ROWS(V, rv, 0, 0, 2);
  PIN();
  u32x2 qg[3];
  const int qgo = WS_Q + (h * 4 + b) * DH * 8 + lane * 8;
#pragma unroll
  for (int j = 0; j < 3; ++j) qg[j] = __builtin_amdgcn_raw_buffer_load_b64(rs, qgo + j * 512, 0, 16);
  PIN();
  LD_ROWS(V, rv, 0, 2, 4);
  PIN();
  u32x4 WS[5], XS;
  {
#pragma unroll
    for (int i = 0; i < 5; ++i) {
      int ch = tid + i * NTHR; ch = ch < 2400 ? ch : 2399;
      const int row = ch / 10, cc = ch - row * 10;
      const u16* p = row < 192 ? wo + (size_t)(h * DH + row) * DM : w2 + (size_t)(h * 48 + row - 192) * DM;
      WS[i] = ld16(p + g * 80 + cc * 8);
    }
    XS = ld16(x + (tid < 320 ? tid : 319) * 8);
  }
  const u16* gp = gw + 10 * lane;
  const u32x4 ga = *reinterpret_cast<const u32x4_a4*>(gp);
  const u32 gb = *reinterpret_cast<const u32*>(gp + 8);
  PIN();
  {
    const u32 tlo = (u32)(tag << 16), thi = (u32)(tag >> 16);
#define QG_OK() __all(((qg[0][0] ^ tlo) >> 16 | (qg[1][0] ^ tlo) >> 16 | (qg[2][0] ^ tlo) >> 16 | (qg[0][1] ^ thi) | (qg[1][1] ^ thi) | (qg[2][1] ^ thi)) == 0u)
    if (!QG_OK()) {
      for (unsigned spins = 0; spins < SPIN_CAP; ++spins) {
#pragma unroll
        for (int j = 0; j < 3; ++j) qg[j] = __builtin_amdgcn_raw_buffer_load_b64(rs, qgo + j * 512, 0, 16);
        if (QG_OK()) break;
        __builtin_amdgcn_s_sleep(1);
      }
    }
#undef QG_OK
#pragma unroll
    for (int j = 0; j < 3; ++j) qs[wave * DH + j * 64 + lane] = (u16)qg[j][0];
  }
  asm volatile("s_waitcnt lgkmcnt(0)" ::: "memory");
  u32x4 q[3];
#pragma unroll
  for (int j = 0; j < 3; ++j) q[j] = *reinterpret_cast<const u32x4*>(&qs[wave * DH + j * 64 + c * 8]);
  constexpr float C2 = 0.07216878364870322f * 1.4426950408889634f;
  float m = -INFINITY, l = 0.f;
#pragma unroll
  for (int k = 0; k < 24; ++k) acc[k] = 0.f;

#define TILE(T_NEXT, HAS_NEXT) do { \
    float s[4]; \
    _Pragma("unroll") for (int i = 0; i < 4; ++i) { float a0 = 0.f, a1 = 0.f, a2 = 0.f; \
      _Pragma("unroll") for (int e = 0; e < 4; ++e) { a0 = dot2(K[i][0][e], q[0][e], a0); a1 = dot2(K[i][1][e], q[1][e], a1); a2 = dot2(K[i][2][e], q[2][e], a2); } \
      s[i] = a0 + a1 + a2; } \
    asm volatile("" : "+v"(s[0]), "+v"(s[1]), "+v"(s[2]), "+v"(s[3])); \
    PIN(); \
    if (HAS_NEXT) { const u64 due = t_start + (u64)(PACE0 + (T_NEXT) * PACE); while (__builtin_amdgcn_s_memrealtime() < due) __builtin_amdgcn_s_sleep(8); } \
    PIN(); \
    if (HAS_NEXT) { LD_TILE(K, rk, T_NEXT); } \
    PIN(); \
    _Pragma("unroll") for (int i = 0; i < 4; ++i) s[i] = csum8(s[i]) * C2; \
    const float mn = fmaxf(fmaxf(fmaxf(s[0], s[1]), fmaxf(s[2], s[3])), m); \
    const float al = __builtin_amdgcn_exp2f(m - mn); m = mn; \
    float p[4]; \
    _Pragma("unroll") for (int i = 0; i < 4; ++i) p[i] = __builtin_amdgcn_exp2f(s[i] - mn); \
    l = fmaf(l, al, (p[0] + p[1]) + (p[2] + p[3])); \
    _Pragma("unroll") for (int k = 0; k < 24; ++k) acc[k] *= al; \
    _Pragma("unroll") for (int i = 0; i < 4; ++i) _Pragma("unroll") for (int j = 0; j < 3; ++j) _Pragma("unroll") for (int e = 0; e < 4; ++e) { \
      acc[j * 8 + 2 * e] = fmaf(p[i], bf_lo(V[i][j][e]), acc[j * 8 + 2 * e]); acc[j * 8 + 2 * e + 1] = fmaf(p[i], bf_hi(V[i][j][e]), acc[j * 8 + 2 * e + 1]); } \
    PIN_ACC(acc); \
    asm volatile("" : "+v"(m), "+v"(l)); \
    PIN(); \
    if (HAS_NEXT) { LD_TILE(V, rv, T_NEXT); } \
    PIN(); \
  } while (0)

  TILE(1, true);

  u32x4 W1[10];
  {
    if (tid < 320) {
      const int bb = tid / 80, k0 = (tid - bb * 80) * 8;
#pragma unroll
      for (int e = 0; e < 4; ++e) { xs[(k0 + 2 * e) * 4 + bb] = bf_lo(XS[e]); xs[(k0 + 2 * e + 1) * 4 + bb] = bf_hi(XS[e]); }
    }
#pragma unroll
    for (int i = 0; i < 5; ++i) {
      const int ch = tid + i * NTHR;
      if (ch < 2400) {
        const int row = ch / 10, cc = ch - row * 10;
#pragma unroll
        for (int e = 0; e < 4; ++e) { wt[(cc * 8 + 2 * e) * WTS + row] = (u16)WS[i][e]; wt[(cc * 8 + 2 * e + 1) * WTS + row] = (u16)(WS[i][e] >> 16); }
      }
    }
    if (tid < 160) *reinterpret_cast<u32x4*>(&wt[(tid >> 1) * WTS + 240 + (tid & 1) * 8]) = u32x4{0u, 0u, 0u, 0u};
    const u32 gv[5] = {ga[0], ga[1], ga[2], ga[3], gb};
    float gwf[10];
#pragma unroll
    for (int i = 0; i < 10; ++i) gwf[i] = (i & 1) ? bf_hi(gv[i >> 1]) : bf_lo(gv[i >> 1]);
    PIN();
    const rsrc_t rw1 = mk_rsrc(w1 + h * 48, (DM * FF - h * 48) * 2);
    const int vo = (10 * r * FF + (c < 6 ? c : 5) * 8) * 2, so = 80 * wave * FF * 2;
#pragma unroll
    for (int i = 0; i < 10; ++i) W1[i] = ld_w(rw1, vo, so + i * FF * 2);
    PIN();
    WG_BARRIER();
    if (wave < 4) {
      float s = 0.f;
#pragma unroll
      for (int i = 0; i < 10; ++i) s += rbf(xs[(10 * lane + i) * 4 + wave] * gwf[i]);
      s = rsum8(csum8(s));
      const float z = rbf(s), gg = rbf(1.f / (1.f + __expf(-z)));
      if (lane == 0) { gs[wave] = gg; gs[4 + wave] = rbf(1.f - gg); }
    }
    PIN();
  }
  TILE(2, true);
  {
    float fa[32];
#pragma unroll
    for (int k = 0; k < 32; ++k) fa[k] = 0.f;
#pragma unroll
    for (int i = 0; i < 10; ++i) {
      const f32x4 xv = *reinterpret_cast<const f32x4*>(&xs[(80 * wave + 10 * r + i) * 4]);
#pragma unroll
      for (int e = 0; e < 4; ++e)
#pragma unroll
        for (int bb = 0; bb < 4; ++bb) {
          fa[bb * 8 + 2 * e] = fmaf(xv[bb], bf_lo(W1[i][e]), fa[bb * 8 + 2 * e]); fa[bb * 8 + 2 * e + 1] = fmaf(xv[bb], bf_hi(W1[i][e]), fa[bb * 8 + 2 * e + 1]); }
    }
#pragma unroll
    for (int k = 0; k < 32; ++k) fa[k] = rsum8(fa[k]);
    if (r == 0 && c < 6) {
#pragma unroll
      for (int bb = 0; bb < 4; ++bb) {
        *reinterpret_cast<f32x4*>(&red[wave * DH + bb * 48 + c * 8]) = f32x4{fa[bb * 8], fa[bb * 8 + 1], fa[bb * 8 + 2], fa[bb * 8 + 3]};
        *reinterpret_cast<f32x4*>(&red[wave * DH + bb * 48 + c * 8 + 4]) = f32x4{fa[bb * 8 + 4], fa[bb * 8 + 5], fa[bb * 8 + 6], fa[bb * 8 + 7]};
      }
    }
    WG_BARRIER();
    if (tid < 192) {
      float s = 0.f;
#pragma unroll
      for (int w = 0; w < 8; ++w) s += red[w * DH + tid];
      const float hv = rbf(s), a = hv / (1.f + __expf(-hv));
      const int bb = tid / 48;
      hs[bb * 64 + tid - bb * 48] = (u16)cvtpk(a, a);
    } else if (tid < 256) hs[((tid - 192) >> 4) * 64 + 48 + (tid & 15)] = 0;
    WG_BARRIER();
    if (wave == 0) {
      const f32x4 omg = *reinterpret_cast<const f32x4*>(&gs[4]);
#pragma unroll
      for (int t = 0; t < 5; ++t) {
        f32x4 a = {0.f, 0.f, 0.f, 0.f};
#pragma unroll
        for (int s = 0; s < 2; ++s) {
          const u32x4 af = *reinterpret_cast<const u32x4*>(&hs[(lane & 3) * 64 + 32 * s + 8 * (lane >> 4)]);
          const u32x4 bf = *reinterpret_cast<const u32x4*>(&wt[(16 * t + (lane & 15)) * WTS + 192 + 32 * s + 8 * (lane >> 4)]);
          a = __builtin_amdgcn_mfma_f32_16x16x32_bf16(__builtin_bit_cast(bf16x8, af), __builtin_bit_cast(bf16x8, bf), a, 0, 0, 0);
        }
        if (lane < 16) *reinterpret_cast<f32x4*>(&fb[(16 * t + lane) * 4]) = a * omg;
      }
    }
    PIN();
  }
#pragma unroll 1
  for (int t = 2; t < NT - 1; ++t) TILE(t + 1, true);
  TILE(0, false);
#undef TILE
#undef LD_TILE
  {
    const float M = rmax8(m), sc = __builtin_amdgcn_exp2f(m - M);
    l = rsum8(l * sc);
#pragma unroll
    for (int k = 0; k < 24; ++k) acc[k] = rsum8(acc[k] * sc);
    if (r == 0) {
#pragma unroll
      for (int j = 0; j < 3; ++j) {
        *reinterpret_cast<f32x4*>(&wp[wave * 196 + j * 64 + c * 8]) = f32x4{acc[j * 8], acc[j * 8 + 1], acc[j * 8 + 2], acc[j * 8 + 3]};
        *reinterpret_cast<f32x4*>(&wp[wave * 196 + j * 64 + c * 8 + 4]) = f32x4{acc[j * 8 + 4], acc[j * 8 + 5], acc[j * 8 + 6], acc[j * 8 + 7]};
      }
      if (c == 0) { wp[wave * 196 + 192] = M; wp[wave * 196 + 193] = l; }
    }
  }
  WG_BARRIER();
  const u32 t40lo = (u32)(tag << 24), t40hi = (u32)(tag >> 8);
#define GR_BAD2(v) ((((v)[0] ^ t40lo) >> 24) | ((v)[1] ^ t40hi) | (((v)[2] ^ t40lo) >> 24) | ((v)[3] ^ t40hi))
  if (tid < 194) {
    float M = -INFINITY;
#pragma unroll
    for (int w = 0; w < 8; ++w) M = fmaxf(M, wp[w * 196 + 192]);
    M = dec24(enc24(M));
    float o = 0.f;
    const int idx = tid < 192 ? tid : 193;
#pragma unroll
    for (int w = 0; w < 8; ++w) o = fmaf(__builtin_amdgcn_exp2f(wp[w * 196 + 192] - M), wp[w * 196 + idx], o);
    if (tid == 192) o = M;
    __hip_atomic_store((gu64*)(ws + WS_P1 + (h * 8 + mem) * 2048 + tid * 8), ((u64)t40hi << 32) | (t40lo | enc24(o)), RLX_AGENT);
  }
  const int tn = wave < 5 ? wave : 4;
  u32x4 bfr[6];
#pragma unroll
  for (int s = 0; s < 6; ++s) bfr[s] = *reinterpret_cast<const u32x4*>(&wt[(16 * tn + (lane & 15)) * WTS + 32 * s + 8 * (lane >> 4)]);
  const float gg = gs[lane >> 4], fbv = fb[(16 * tn + (lane & 15)) * 4 + (lane >> 4)];
  if (wave < 4) {
    const int o0 = WS_P1 + (h * 8 + 2 * wave) * 2048 + lane * 16, o1 = WS_P1 + (h * 8 + 2 * wave) * 2048 + 1024 + (lane < 33 ? lane : 32) * 16;
    u32x4 pa0, pb0, pa1, pb1;
    for (unsigned spins = 0; spins < SPIN_CAP; ++spins) {
      pa0 = __builtin_amdgcn_raw_buffer_load_b128(rs, o0, 0, 16); pb0 = __builtin_amdgcn_raw_buffer_load_b128(rs, o1, 0, 16);
      pa1 = __builtin_amdgcn_raw_buffer_load_b128(rs, o0 + 2048, 0, 16); pb1 = __builtin_amdgcn_raw_buffer_load_b128(rs, o1 + 2048, 0, 16);
      if (__all((GR_BAD2(pa0) | GR_BAD2(pb0) | GR_BAD2(pa1) | GR_BAD2(pb1)) == 0u)) break;
      __builtin_amdgcn_s_sleep(POLL_SLEEP);
    }
    const float M0 = dec24(__builtin_amdgcn_readlane(pb0[0], 32)), L0 = dec24(__builtin_amdgcn_readlane(pb0[2], 32));
    const float M1 = dec24(__builtin_amdgcn_readlane(pb1[0], 32)), L1 = dec24(__builtin_amdgcn_readlane(pb1[2], 32));
    const float Mb = fmaxf(M0, M1), w0 = __builtin_amdgcn_exp2f(M0 - Mb), w1_ = __builtin_amdgcn_exp2f(M1 - Mb);
    const float inv = 1.f / fmaf(w0, L0, w1_ * L1), u0 = w0 * inv, u1 = w1_ * inv;
    *reinterpret_cast<u32*>(&us[wave * DH + 2 * lane]) =
        cvtpk(fmaf(dec24(pa0[0]), u0, dec24(pa1[0]) * u1), fmaf(dec24(pa0[2]), u0, dec24(pa1[2]) * u1));
    if (lane < 32) *reinterpret_cast<u32*>(&us[wave * DH + 128 + 2 * lane]) =
        cvtpk(fmaf(dec24(pb0[0]), u0, dec24(pb1[0]) * u1), fmaf(dec24(pb0[2]), u0, dec24(pb1[2]) * u1));
  }
  WG_BARRIER();
  if (wave < 5) {
    f32x4 a = {0.f, 0.f, 0.f, 0.f};
#pragma unroll
    for (int s = 0; s < 6; ++s) {
      const u32x4 af = *reinterpret_cast<const u32x4*>(&us[(lane & 3) * DH + 32 * s + 8 * (lane >> 4)]);
      a = __builtin_amdgcn_mfma_f32_16x16x32_bf16(__builtin_bit_cast(bf16x8, af), __builtin_bit_cast(bf16x8, bfr[s]), a, 0, 0, 0);
    }
    const int bb = lane >> 4;
    const float av = bb == 0 ? a[0] : bb == 1 ? a[1] : bb == 2 ? a[2] : a[3];
    const float res = fmaf(av, gg, fbv);
    const int n = 16 * wave + (lane & 15), ck = n / 10;
    __hip_atomic_store((gu64*)(ws + WS_P2 + ((g * 4 + bb) * 8 + ck) * 2560 + h * 80 + (n - ck * 10) * 8), ((u64)t40hi << 32) | (t40lo | enc24(res)), RLX_AGENT);
  }
  if (wave != 0) return;
  {
    const int ob = h >> 3, n0 = (h & 7) * 10, hf = lane >> 5;
    const int po = WS_P2 + ((g * 4 + ob) * 8 + (h & 7)) * 2560 + (lane & 31) * 80 + hf * 48;
    u32x4 p0, p1, p2;
    for (unsigned spins = 0; spins < SPIN_CAP; ++spins) {
      p0 = __builtin_amdgcn_raw_buffer_load_b128(rs, po, 0, 16);
      p1 = __builtin_amdgcn_raw_buffer_load_b128(rs, po + 16, 0, 16);
      p2 = __builtin_amdgcn_raw_buffer_load_b128(rs, po + (hf ? 16 : 32), 0, 16);
      if (__all((GR_BAD2(p0) | GR_BAD2(p1) | GR_BAD2(p2)) == 0u)) break;
      __builtin_amdgcn_s_sleep(POLL_SLEEP);
    }
    float v[6] = {dec24(p0[0]), dec24(p0[2]), dec24(p1[0]), dec24(p1[2]), dec24(p2[0]), dec24(p2[2])};
#pragma unroll
    for (int k = 0; k < 6; ++k) {
      float t = csum8(v[k]);
      t += dpp<ROR8>(t);
      auto sw = __builtin_amdgcn_permlane16_swap(__float_as_uint(t), __float_as_uint(t), false, false);
      v[k] = __uint_as_float(sw[0]) + __uint_as_float(sw[1]);
    }
    const int pi = lane & 31;
    const u32 pk = pi == 0 ? cvtpk(v[0], v[1]) : pi == 1 ? cvtpk(v[2], v[3]) : cvtpk(v[4], v[5]);
    if (pi < (hf ? 2 : 3)) *reinterpret_cast<u32*>(out + ob * DM + g * 80 + n0 + hf * 6 + pi * 2) = pk;
    if (bid == 0 && lane == 0) __hip_atomic_store((gu64*)(ws + WS_EPOCH), tag, RLX_AGENT);
  }
#undef GR_BAD2
}

extern "C" void kernel_launch(void* const* d_in, const int* in_sizes, int n_in, void* d_out, int out_size, void* d_ws, size_t ws_size,
                              hipStream_t stream) {
  (void)in_sizes; (void)n_in; (void)out_size; (void)ws_size;
  fused_decode<<<256, NTHR, 0, stream>>>((const u16*)d_in[0], (const u16*)d_in[1], (const u16*)d_in[2], (const u16*)d_in[3], (const u16*)d_in[4],
                                         (const u16*)d_in[6], (const u16*)d_in[7], (const u16*)d_in[8], (u16*)d_out, (unsigned char*)d_ws);
}
```
